```python
import torch

def _softmax(x, axis=-1):
    return torch.softmax(x, dim=axis)
def _gelu(x):
    return torch.nn.functional.gelu(x, approximate="tanh")
def _silu(x):
    return torch.nn.functional.silu(x)
def _sigmoid(x):
    return torch.sigmoid(x)
def _layernorm(x, axis=-1, eps=1e-5):
    m = x.mean(dim=axis, keepdim=True)
    v = x.var(dim=axis, keepdim=True, unbiased=False)
    return (x - m) / torch.sqrt(v + eps)
def _scatter_add(tgt, idx, src, axis=0):
    return tgt.clone().index_add_(axis, idx, src)
def _segment_sum(src, seg, n):
    out = torch.zeros((n,) + src.shape[1:], dtype=src.dtype)
    return out.index_add_(0, seg, src)
def _pad(x, pw):
    flat = tuple(v for pair in reversed(pw) for v in pair)
    return torch.nn.functional.pad(x, flat)

kernel_name = 'concat_heads__mha_block_cached__gqa_block_cached__near_ridge'

def setup_inputs():

    _w = (lambda a: (lambda t: t.to(torch.bfloat16) if t.is_floating_point() else t)(torch.as_tensor(a)))
    def _A_setup():
        B, Sq, D = 16, 48, 640
        H, Sk, Dh = 32, 48, 128
        return {
            "x": _w((lambda *s: torch.randn(*s, dtype=torch.bfloat16) * 0.8)(B, Sq, D)),
            "k_cache": _w((lambda *s: torch.randn(*s, dtype=torch.bfloat16) * 0.8)(B, H, Sk, Dh)),
            "v_cache": _w((lambda *s: torch.randn(*s, dtype=torch.bfloat16) * 0.05)(B, H, Sk, Dh)),
            "wq": _w((lambda *s: torch.randn(*s, dtype=torch.bfloat16) * 0.8)(D, H * Dh) * (D ** -0.5)),
            "wo": _w((lambda *s: torch.randn(*s, dtype=torch.bfloat16) * 0.05)(H * Dh, D) * ((H * Dh) ** -0.5)),
        }
    _A_in = _A_setup()

    def _B_setup():
        B, Sq, D = 16, 48, 640
        Hq, Hkv, Sk, Dh = 32, 32, 48, 128
        assert Hq % Hkv == 0, (Hq, Hkv)
        return {
            "x": _w((lambda *s: torch.randn(*s, dtype=torch.bfloat16) * 0.8)(B, Sq, D)),
            "k_cache": _w((lambda *s: torch.randn(*s, dtype=torch.bfloat16) * 0.8)(B, Hkv, Sk, Dh)),
            "v_cache": _w((lambda *s: torch.randn(*s, dtype=torch.bfloat16) * 0.05)(B, Hkv, Sk, Dh)),
            "wq": _w((lambda *s: torch.randn(*s, dtype=torch.bfloat16) * 0.8)(D, Hq * Dh) * (D ** -0.5)),
            "wo": _w((lambda *s: torch.randn(*s, dtype=torch.bfloat16) * 0.05)(Hq * Dh, D) * ((Hq * Dh) ** -0.5)),
        }
    _B_in = _B_setup()

    return {
        "A_x": _A_in["x"],
        "A_k_cache": _A_in["k_cache"],
        "A_v_cache": _A_in["v_cache"],
        "A_wq": _A_in["wq"],
        "A_wo": _A_in["wo"],
        "B_x": _B_in["x"],
        "B_k_cache": _B_in["k_cache"],
        "B_v_cache": _B_in["v_cache"],
        "B_wq": _B_in["wq"],
        "B_wo": _B_in["wo"],
    }

def reference(A_x, A_k_cache, A_v_cache, A_wq, A_wo, B_x, B_k_cache, B_v_cache, B_wq, B_wo):
    def _A_ref(x, k_cache, v_cache, wq, wo):
        B, Sq, D = x.shape
        H, Dh = 32, 128
        q = (lambda x, *d: x.permute(*d))((x @ wq).reshape(B, Sq, H, Dh), 0, 2, 1, 3)
        scores = torch.einsum("bhqd,bhkd->bhqk", q, k_cache) * (Dh ** -0.5)
        p = _softmax(scores, axis=-1)
        o = torch.einsum("bhqk,bhkd->bhqd", p, v_cache)
        return (lambda x, *d: x.permute(*d))(o, 0, 2, 1, 3).reshape(B, Sq, H * Dh) @ wo

    def _B_ref(x, k_cache, v_cache, wq, wo):
        B, Sq, D = x.shape
        Hq, Dh = 32, 128
        Hkv = k_cache.shape[1]
        rep = Hq // Hkv
        q = (lambda x, *d: x.permute(*d))((x @ wq).reshape(B, Sq, Hq, Dh), 0, 2, 1, 3)
        k = (lambda x, n, axis: torch.repeat_interleave(x, n, dim=axis))(k_cache, rep, axis=1)
        v = (lambda x, n, axis: torch.repeat_interleave(x, n, dim=axis))(v_cache, rep, axis=1)
        scores = torch.einsum("bhqd,bhkd->bhqk", q, k) * (Dh ** -0.5)
        p = _softmax(scores, axis=-1)
        o = torch.einsum("bhqk,bhkd->bhqd", p, v)
        return (lambda x, *d: x.permute(*d))(o, 0, 2, 1, 3).reshape(B, Sq, Hq * Dh) @ wo

    a = _A_ref(A_x, A_k_cache, A_v_cache, A_wq, A_wo)
    b = _B_ref(B_x, B_k_cache, B_v_cache, B_wq, B_wo)
    return torch.concatenate([a, b], axis=-1)
```

```cpp
#include <hip/hip_runtime.h>
#include <type_traits>

typedef float f32x4 __attribute__((ext_vector_type(4)));
typedef float f32x2 __attribute__((ext_vector_type(2)));
typedef __bf16 bf16x8 __attribute__((ext_vector_type(8)));
typedef __bf16 bf16x2 __attribute__((ext_vector_type(2)));
typedef short s16x4 __attribute__((ext_vector_type(4)));
typedef unsigned u32x4 __attribute__((ext_vector_type(4)));
typedef unsigned u32x2 __attribute__((ext_vector_type(2)));
typedef unsigned short u16;
typedef unsigned long long u64;
#define LAS __attribute__((address_space(3)))
#define GAS __attribute__((address_space(1)))
#define DI __device__ __forceinline__
typedef __amdgpu_buffer_rsrc_t rsrc_t;

constexpr int AUX_SC1 = 16;
constexpr size_t WS_PART = 16u << 20, WS_CTRL = 40u << 20;
constexpr unsigned SPIN_CAP = 1u << 15;
constexpr int HD = 185, HDU = 6, HDB = 18;

constexpr unsigned SLOT_B = 40960;
constexpr unsigned KVBUF = 131072;
constexpr unsigned K2BUF = 40960;
constexpr unsigned UNIT_B = 32768;
constexpr unsigned OFFBUF = 122880;

DI rsrc_t mk_rsrc(const void* p, unsigned bytes) {
  return __builtin_amdgcn_make_buffer_rsrc((void*)p, (short)0, (int)bytes, 0x00020000);
}
DI void dma16(unsigned lds_base, unsigned imm, unsigned voff, rsrc_t rs, unsigned soff) {
  asm volatile("s_add_u32 m0, %0, %4\n\ts_nop 0\n\tbuffer_load_dwordx4 %1, %2, %3 offen lds"
               :: "s"(lds_base), "v"(voff), "s"(rs), "s"(soff), "i"(imm) : "memory", "scc");
}
template <int N> DI void wait_vm() {
  asm volatile("" ::: "memory");
  __builtin_amdgcn_s_waitcnt((N & 15) | (7 << 4) | (0 << 8) | ((N >> 4) << 14));
  asm volatile("" ::: "memory");
}
template <int N> DI void wait_bar() {
  wait_vm<N>();
  __builtin_amdgcn_s_barrier();
  asm volatile("" ::: "memory");
}
#define SCHED0() __builtin_amdgcn_sched_barrier(0)
template <int I, int N, class F> DI void static_for(F&& f) {
  if constexpr (I < N) { f(std::integral_constant<int, I>{}); static_for<I + 1, N>(f); }
}
DI unsigned cvtpk(float lo, float hi) {
  f32x2 v = {lo, hi};
  bf16x2 b = __builtin_convertvector(v, bf16x2);
  return __builtin_bit_cast(unsigned, b);
}
DI f32x4 mfma32(u32x4 a, u32x4 b, f32x4 c) {
  return __builtin_amdgcn_mfma_f32_16x16x32_bf16(__builtin_bit_cast(bf16x8, a), __builtin_bit_cast(bf16x8, b), c, 0, 0, 0);
}
DI u32x2 trread(unsigned addr) {
  s16x4 r = __builtin_amdgcn_ds_read_tr16_b64_v4i16((LAS s16x4*)addr);
  return __builtin_bit_cast(u32x2, r);
}
DI u32x4 ldsread16(unsigned addr) { return *(LAS u32x4*)addr; }
DI void ldswrite16(unsigned addr, u32x4 v) { *(LAS u32x4*)addr = v; }
DI float max2f(float a, float b) { return __builtin_elementwise_maximum(a, b); }
DI float max3f(float a, float b, float c) { return max2f(max2f(a, b), c); }

struct Frags { u32x4 a[8]; u32x4 b[3]; };
struct FragsB { u32x4 v[3]; u32x4 wo[5]; };

DI void softmax_unit(const f32x4 (&st)[3], u32x4& pa, u32x2& pc) {
  const float cs = 0.08838834764831845f * 1.4426950408889634f;
  float mx = max3f(max3f(st[0][0], st[0][1], st[0][2]), max3f(st[0][3], st[1][0], st[1][1]),
                   max3f(max3f(st[1][2], st[1][3], st[2][0]), max3f(st[2][1], st[2][2], st[2][3]), st[0][0]));
  {
    auto r = __builtin_amdgcn_permlane16_swap(__float_as_uint(mx), __float_as_uint(mx), false, false);
    mx = max2f(__uint_as_float(r[0]), __uint_as_float(r[1]));
    auto r2 = __builtin_amdgcn_permlane32_swap(__float_as_uint(mx), __float_as_uint(mx), false, false);
    mx = max2f(__uint_as_float(r2[0]), __uint_as_float(r2[1]));
  }
  const float mc = mx * cs;
  float e[3][4];
#pragma unroll
  for (int kt = 0; kt < 3; ++kt)
#pragma unroll
    for (int r = 0; r < 4; ++r) e[kt][r] = __builtin_amdgcn_exp2f(st[kt][r] * cs - mc);
  float sum = ((e[0][0] + e[0][1]) + (e[0][2] + e[0][3])) + ((e[1][0] + e[1][1]) + (e[1][2] + e[1][3])) +
              ((e[2][0] + e[2][1]) + (e[2][2] + e[2][3]));
  {
    auto r = __builtin_amdgcn_permlane16_swap(__float_as_uint(sum), __float_as_uint(sum), false, false);
    sum = __uint_as_float(r[0]) + __uint_as_float(r[1]);
    auto r2 = __builtin_amdgcn_permlane32_swap(__float_as_uint(sum), __float_as_uint(sum), false, false);
    sum = __uint_as_float(r2[0]) + __uint_as_float(r2[1]);
  }
  const float inv = __builtin_amdgcn_rcpf(sum);
  pa = u32x4{cvtpk(e[0][0] * inv, e[0][1] * inv), cvtpk(e[0][2] * inv, e[0][3] * inv),
             cvtpk(e[1][0] * inv, e[1][1] * inv), cvtpk(e[1][2] * inv, e[1][3] * inv)};
  pc = u32x2{cvtpk(e[2][0] * inv, e[2][1] * inv), cvtpk(e[2][2] * inv, e[2][3] * inv)};
}
DI void s_unit(const u32x4 (&kf)[3][4], const u32x4 (&qf)[4], f32x4 (&st)[3]) {
#pragma unroll
  for (int kt = 0; kt < 3; ++kt) st[kt] = f32x4{0.f, 0.f, 0.f, 0.f};
#pragma unroll
  for (int s = 0; s < 4; ++s)
#pragma unroll
    for (int kt = 0; kt < 3; ++kt) st[kt] = mfma32(kf[kt][s], qf[s], st[kt]);
}

DI int fresh_lane() {
  int l;
  asm volatile("v_mbcnt_lo_u32_b32 %0, -1, 0\n\tv_mbcnt_hi_u32_b32 %0, -1, %0" : "=v"(l));
  return l;
}
struct Params {
  const u16* in[2][5];
  u16* out;
  char* ws;
};
DI char* mkptr(unsigned lo, unsigned hi) { return (char*)(((u64)hi << 32) | lo); }

DI void mfa(f32x4& c, const u32x4& a, const u32x4& b) { asm volatile("v_mfma_f32_16x16x32_bf16 %0, %1, %2, %0" : "+v"(c) : "v"(a), "v"(b)); }
DI void mfz(f32x4& c, const u32x4& a, const u32x4& b) { asm volatile("v_mfma_f32_16x16x32_bf16 %0, %1, %2, 0" : "=&v"(c) : "v"(a), "v"(b)); }
DI unsigned cvtpk_v(float lo, float hi) { unsigned r; asm volatile("v_cvt_pk_bf16_f32 %0, %1, %2" : "=v"(r) : "v"(lo), "v"(hi)); return r; }
template <class T> DI T zero16() {
  u32x2 a, b;
  asm volatile("v_mov_b64 %0, 0" : "=v"(a));
  asm volatile("v_mov_b64 %0, 0" : "=v"(b));
  return __builtin_bit_cast(T, u32x4{a[0], a[1], b[0], b[1]});
}
#define NOP8() asm volatile("s_nop 7\n\ts_nop 1")

__global__ __launch_bounds__(512, 2) void fused_kernel(Params P) {
  u32x4 aow, aowA;
  unsigned warm;
  const u64 ka = (u64)__builtin_amdgcn_kernarg_segment_ptr();
  asm volatile("s_load_dword %1, %2, 0x0\n\ts_load_dwordx4 %0, %2, 0x50" : "=&s"(aowA), "=&s"(warm) : "s"(ka));
  SCHED0();
  const int bid = blockIdx.x, xcd = bid & 7, idx = bid >> 3;
  const int br = xcd >> 2, hg = xcd & 3, j8 = idx & 7, h = hg * 8 + j8, bg = idx >> 3;
  const int w = __builtin_amdgcn_readfirstlane(threadIdx.x >> 6);
  const int b4 = w & 3, hf = w >> 2;
  const int b = bg * 4 + b4;
  const int c4 = (j8 + bg) & 3;
  auto stage_of = [&](int n0) {
    const int lo0 = n0 - ((n0 - (1 + (hg & 1))) & 3), lo = lo0 < 0 ? 0 : lo0, sz = (lo0 + 4 > 10 ? 10 : lo0 + 4) - lo;
    int pos = n0 - lo + ((c4 * sz) >> 2);
    if (pos >= sz) pos -= sz;
    return lo + pos;
  };
  char* ws;
  char* ctrl;
  rsrc_t rsP;
  unsigned tag;
  auto epoch_issue = [&]() { asm volatile("s_load_dword %0, %1, 0x0" : "=s"(tag) : "s"(ctrl + (br * 4 + bg) * 256) : "memory"); };
  auto epoch_done = [&]() { asm volatile("s_waitcnt lgkmcnt(0)" : "+s"(tag) :: "memory"); tag += 1u; };
  const int bb = b4 >> 1, nh = b4 & 1, cq = j8 & 3;
  const int b2 = bg * 4 + 2 * (j8 >> 2) + bb, c8 = 2 * cq + nh;
  auto ws_setup = [&]() {
    asm volatile("" : "+s"(aow));
    ws = mkptr(aow[2], aow[3]);
    ctrl = ws + WS_CTRL;
    rsP = mk_rsrc(ws + WS_PART + (size_t)((br * 16 + b2) * 8 + c8) * 41216, 40960 + 32);
  };
  float own[12];
  u32x2 hint;
#define LANE_DEFS \
  const int lane = fresh_lane(); \
  const int g = lane >> 4, q = (lane >> 2) & 3, p = lane & 3, m = lane & 15;
  constexpr unsigned PA_AREA = KVBUF, PC_AREA = KVBUF + 18432;

  if (hf == 0) {
    asm volatile("s_waitcnt lgkmcnt(0)" : "+s"(aowA) : "s"(warm) : "memory");
    aow = aowA;
    ws_setup();
    epoch_issue();
    const rsrc_t rsPb = mk_rsrc(ws + (size_t)((br * 16 + b) * 32 + h) * 6144, 6144);
    LANE_DEFS
    {
      unsigned vv[2], wov[5];
#pragma unroll
      for (int e = 0; e < 2; ++e) vv[e] = (lane >> 3) * 256 + (((lane & 7) ^ ((4 * (e ^ nh) + g) & 7)) * 16);
#pragma unroll
      for (int i = 0; i < 5; ++i) {
        const int Pq = 64 * i + lane, R = Pq / 20, c = Pq - R * 20;
        int cs = c - 2 * ((R >> 2) & 1);
        if (cs < 0) cs += 20;
        const int hi = cs >= 10 ? 10 : 0, ul = (cs - hi) >> 1;
        cs = hi + 2 * (ul == 0 ? 4 : (hg + ul) & 3) + (cs & 1);
        wov[i] = (8 * (R >> 2) + (R & 3)) * 1280 + cs * 16;
      }
      ldswrite16(OFFBUF + b4 * 2048 + lane * 16, u32x4{wov[0], wov[1], wov[2], wov[3]});
      ldswrite16(OFFBUF + b4 * 2048 + 1024 + lane * 16, u32x4{wov[4], vv[0], vv[1], 0u});
    }
    unsigned ax[2];
#pragma unroll
    for (int v = 0; v < 2; ++v) ax[v] = 16384 + b4 * 6144 + m * 128 + 16 * ((4 * v + g) ^ (m >> 1));
    unsigned sb = 0;
    auto next_slot = [&]() { sb = (sb == 2 * SLOT_B) ? 0u : sb + SLOT_B; };
    unsigned ub = 2 * UNIT_B;
    auto next_unit = [&]() { ub = (ub + UNIT_B) & (4 * UNIT_B - 1); asm volatile("" : "+s"(ub)); };
    const unsigned awo = (4 * g + q) * 320 + 8 * p + 32 * (g & 1) + 160 * nh;
    const unsigned awo4 = (nh && (g & 1)) ? (4 * g + q) * 320 + 8 * p : awo + 128;
    unsigned av[2];
#pragma unroll
    for (int v = 0; v < 2; ++v) av[v] = 20480 + bb * 6144 + m * 128 + 16 * ((4 * v + g) ^ (m >> 1));
    auto read_b = [&](FragsB& f, int kk) {
      const unsigned x0 = av[kk] + ub, w0 = awo + ub, w4 = awo4 + ub;
#pragma unroll
      for (int kt = 0; kt < 3; ++kt) f.v[kt] = ldsread16(x0 + kt * 2048);
#pragma unroll
      for (int u = 0; u < 5; ++u) {
        const unsigned a = (u == 4 ? w4 : w0 + 32 * u) + 10240 * kk;
        u32x2 lo = trread(a), hi = trread(a + 5120);
        f.wo[u] = u32x4{lo[0], lo[1], hi[0], hi[1]};
      }
    };
    FragsB g0, g1;
    {
      unsigned aw[2];
#pragma unroll
      for (int v = 0; v < 2; ++v) aw[v] = 2048 * g + 64 * q + 16 * (p ^ (g & 1)) + 8 * ((p + v) & 1);
      unsigned bx[2], w0, w1;
      auto bases = [&]() {
        bx[0] = ax[0] + sb; bx[1] = ax[1] + sb; w0 = aw[0] + sb; w1 = aw[1] + sb;
        asm volatile("" : "+v"(bx[0]), "+v"(bx[1]), "+v"(w0), "+v"(w1));
      };
      auto read_frags = [&](Frags& f, int kk) {
        const unsigned x0 = bx[kk];
#pragma unroll
        for (int mt = 0; mt < 3; ++mt) f.b[mt] = ldsread16(x0 + mt * 2048);
#pragma unroll
        for (int u = 0; u < 8; ++u) {
          const unsigned a = ((u & 1) ? w1 : w0) + 8192 * kk + 256 * (u >> 1);
          u32x2 lo = trread(a), hi = trread(a + 1024);
          f.a[u] = u32x4{lo[0], lo[1], hi[0], hi[1]};
        }
      };
      f32x4 acc[8][3];
#pragma unroll
      for (int u = 0; u < 8; ++u)
#pragma unroll
        for (int mt = 0; mt < 3; ++mt) acc[u][mt] = zero16<f32x4>();
      auto mma = [&](const Frags& f) {
#pragma unroll
        for (int u = 0; u < 8; ++u)
#pragma unroll
          for (int mt = 0; mt < 3; ++mt) acc[u][mt] = mfma32(f.a[u], f.b[mt], acc[u][mt]);
      };
      auto sgb = [&]() {
        static_for<0, 19>([&](auto) { __builtin_amdgcn_sched_group_barrier(0x8, 1, 0); __builtin_amdgcn_sched_group_barrier(0x100, 1, 0); });
        __builtin_amdgcn_sched_group_barrier(0x8, 5, 0);
      };
      Frags f0, f1;
      epoch_done();
      bases();
      wait_bar<0>();
      read_frags(f0, 0);
      wait_vm<0>();
      SCHED0();
      read_frags(f1, 1);
      mma(f0);
      sgb();
      SCHED0();
      next_slot();
      bases();
      wait_vm<63>();
#pragma unroll 1
      for (int i = 0; i < 9; ++i) {
        wait_bar<63>();
        read_frags(f0, 0);
        mma(f1);
        sgb();
        SCHED0();
        read_frags(f1, 1);
        mma(f0);
        sgb();
        SCHED0();
        if (i < 4) __builtin_amdgcn_s_sleep(4);
        next_slot();
        bases();
        wait_vm<63>();
      }
      wait_bar<63>();
      u32x4 kf[3][4];
      {
        const unsigned k0 = b4 * 4096 + m * 256 + 16 * (g ^ m) + 8 * (g & 1);
        unsigned ka[4][2];
#pragma unroll
        for (int s = 0; s < 4; ++s) { ka[s][0] = (k0 ^ (64 * s)) + KVBUF; ka[s][1] = ka[s][0] ^ 8; }
        SCHED0();
        static_for<0, 24>([&](auto IC_) {
          constexpr int i = decltype(IC_)::value, mt = i >> 3, u = i & 7, kt = i >> 3, s = (i >> 1) & 3, hi = i & 1;
          acc[u][mt] = mfma32(f1.a[u], f1.b[mt], acc[u][mt]);
          const u32x2 t = *(volatile LAS u32x2*)(kt < 2 ? ka[s][hi] + kt * 16384 : ka[s][hi] - (KVBUF - K2BUF));
          kf[kt][s][2 * hi] = t[0]; kf[kt][s][2 * hi + 1] = t[1];
          SCHED0();
        });
      }
      f32x4 st[3][3];
#pragma unroll
      for (int mt = 0; mt < 3; ++mt) {
        u32x4 qf[4];
#pragma unroll
        for (int s = 0; s < 4; ++s)
          qf[s] = u32x4{cvtpk(acc[2 * s][mt][0], acc[2 * s][mt][1]), cvtpk(acc[2 * s][mt][2], acc[2 * s][mt][3]),
                        cvtpk(acc[2 * s + 1][mt][0], acc[2 * s + 1][mt][1]), cvtpk(acc[2 * s + 1][mt][2], acc[2 * s + 1][mt][3])};
        s_unit(kf, qf, st[mt]);
      }
#pragma unroll
      for (int mt = 0; mt < 3; ++mt) {
        u32x4 pa; u32x2 pc;
        softmax_unit(st[mt], pa, pc);
        __builtin_amdgcn_raw_buffer_store_b128(u32x4{pa[0], pa[1], pa[2], tag}, rsPb, lane * 16 + mt * 2048, 0, AUX_SC1);
        __builtin_amdgcn_raw_buffer_store_b128(u32x4{pc[0], pc[1], pa[3], tag}, rsPb, lane * 16 + mt * 2048 + 1024, 0, AUX_SC1);
      }
      SCHED0();
      wait_bar<63>();
      read_b(g0, 0);
      SCHED0();
    }
    f32x4 oacc[5][3];
    f32x4 vw[3][5];
    u32x4 wa[2][5], wc[5];
#pragma unroll
    for (int u = 0; u < 5; ++u) {
      wa[1][u] = zero16<u32x4>();
      wc[u] = zero16<u32x4>();
#pragma unroll
      for (int mt = 0; mt < 3; ++mt) oacc[u][mt] = zero16<f32x4>();
    }
    wait_vm<63>();
    SCHED0();
    const unsigned pl = bb * 3072 + lane * 16;
    auto vstep = [&](const FragsB& f, bool first, FragsB& n, int kk, u32x4* pdst = nullptr, unsigned pbase = 0) {
      unsigned x0 = av[kk] + ub, w0 = awo + ub, w4 = awo4 + ub;
      if (pdst) asm volatile("" : "+s"(pbase));
      const unsigned pb0 = pbase + pl;
      SCHED0();
      static_for<0, 15>([&](auto IC_) {
        constexpr int i = decltype(IC_)::value, u = i / 3, kt = i % 3;
        if (first) mfz(vw[kt][u], f.v[kt], f.wo[u]); else mfa(vw[kt][u], f.v[kt], f.wo[u]);
        if constexpr (i < 3) n.v[i] = ldsread16(x0 + i * 2048);
        else if constexpr (i < 13) {
          constexpr int uu = (i - 3) >> 1, hi = (i - 3) & 1;
          const u32x2 t = trread((uu == 4 ? w4 : w0 + 32 * uu) + 10240 * kk + 5120 * hi);
          n.wo[uu][2 * hi] = t[0]; n.wo[uu][2 * hi + 1] = t[1];
        }
        if constexpr (i == 6 || i == 8 || i == 10) { if (pdst) pdst[(i - 6) / 2] = ldsread16(pb0 + (i - 6) / 2 * 1024); }
        SCHED0();
      });
    };
    unsigned l16 = 0, tagv = 0;
    auto st_chunk = [&](int u, int j) {
      const f32x4 x0 = oacc[u][j], x1 = oacc[u][j + 1];
      const u32x4 v = j == 0 ? u32x4{cvtpk_v(x0[0], x0[1]), cvtpk_v(x0[2], x0[3]), cvtpk_v(x1[0], x1[1]), tagv}
                             : u32x4{cvtpk_v(x0[2], x0[3]), cvtpk_v(x1[0], x1[1]), cvtpk_v(x1[2], x1[3]), tagv};
      __builtin_amdgcn_raw_buffer_store_b128(v, rsP, l16, (u == 0 ? 4 : (hg + u) & 3) * 8192 + hg * 2048 + j * 1024, AUX_SC1);
    };
    unsigned par = 2;
    auto unit = [&](bool first, u32x4* pdst = nullptr, unsigned pbase = 0) {
      vstep(g0, first, g1, 1);
      next_unit();
      wait_bar<63>();
      vstep(g1, false, g0, 0, pdst, pbase);
    };
    auto cv = [&](int j, int e, bool do_a, bool do_c) {
      if (do_a && j < 20) {
        const int u = j >> 2, d = j & 3;
        wa[e][u][d] = cvtpk_v(vw[d >> 1][u][2 * (d & 1)], vw[d >> 1][u][2 * (d & 1) + 1]);
      } else if (do_c) {
        const int jj = do_a ? j - 20 : j;
        if (jj < 10) { const int u = jj >> 1, d = jj & 1; wc[u][2 * e + d] = cvtpk_v(vw[2][u][2 * d], vw[2][u][2 * d + 1]); }
      }
    };
    auto fblock = [&](const u32x4 (&src)[5], const u32x4 (&pf)[3], int e, bool do_a, bool do_c, u32x4* rdst, unsigned rbase) {
      asm volatile("" : "+s"(rbase));
      const unsigned rb0 = rbase + pl;
      SCHED0();
      static_for<0, 15>([&](auto IC_) {
        constexpr int i = decltype(IC_)::value;
        mfa(oacc[i / 3][i % 3], src[i / 3], pf[i % 3]);
        cv(2 * i, e, do_a, do_c);
        cv(2 * i + 1, e, do_a, do_c);
        if (rdst && i >= 10 && i < 13) rdst[i - 10] = ldsread16(rb0 + (i - 10) * 1024);
        SCHED0();
      });
    };
    auto rd_p = [&](u32x4 (&pf)[3], unsigned base) {
#pragma unroll
      for (int i = 0; i < 3; ++i) pf[i] = ldsread16(base + pl + i * 1024);
    };
#pragma unroll 1
    for (int k = 0; k < 4; ++k) {
      u32x4 pf[3], pg[3];
      unit(true);
      unit(false, pg, PC_AREA + ((k + 1) & 1) * 6144);
      fblock(wc, pg, 0, true, false, pf, PA_AREA + par * 6144);
      par = par == 2 ? 0 : par + 1;
      fblock(wa[1], pf, 0, false, true, nullptr, 0);
      unit(true);
      unit(false, pf, PA_AREA + par * 6144);
      par = par == 2 ? 0 : par + 1;
      fblock(wa[0], pf, 1, true, true, nullptr, 0);
      wait_vm<63>();
    }
    {
      u32x4 pf[3], pg[3];
      rd_p(pg, PC_AREA + 6144);
      rd_p(pf, PA_AREA + par * 6144);
      NOP8();
      l16 = fresh_lane() * 16;
      asm volatile("v_mov_b32 %0, %1" : "=v"(tagv) : "s"(tag));
      asm volatile("buffer_load_dwordx2 %0, %1, %2, 0 offen sc1" : "=&v"(hint) : "v"((l16 >> 1) + 40960), "s"(rsP) : "memory");
      SCHED0();
      static_for<0, 15>([&](auto IC_) {
        constexpr int T = decltype(IC_)::value;
        mfa(oacc[T / 3][T % 3], wa[1][T / 3], pf[T % 3]);
        mfa(oacc[T / 3][T % 3], wc[T / 3], pg[T % 3]);
        if constexpr (T >= 4 && (T - 4) % 3 < 2) st_chunk((T - 4) / 3, (T - 4) % 3);
        SCHED0();
      });
      NOP8();
      SCHED0();
    }
#pragma unroll
    for (int e = 0; e < 12; ++e) own[e] = oacc[4][e >> 2][e & 3];
    asm volatile("s_waitcnt vmcnt(8)" : "+v"(hint) :: "memory");
  } else {
    u32x4 a01, a23, aowB;
    u32x2 a4;
    asm volatile("s_load_dwordx4 %0, %4, 0x0\n\ts_load_dwordx4 %3, %5, 0x50\n\ts_load_dwordx4 %1, %4, 0x10\n\ts_load_dwordx2 %2, %4, 0x20"
                 : "=&s"(a01), "=&s"(a23), "=&s"(a4), "=&s"(aowB) : "s"(ka + br * 40), "s"(ka));
    unsigned wv[2], xv[2];
    {
      const int lane = fresh_lane();
      const int g = lane >> 4, q = (lane >> 2) & 3, p = lane & 3;
#pragma unroll
      for (int e = 0; e < 2; ++e) {
        wv[e] = q * 8192 + (4 * g + (p ^ e)) * 16;
        xv[e] = (lane >> 3) * 1280 + (((lane & 7) ^ ((4 * e + g) & 7)) * 16);
      }
    }
    unsigned wo0 = h * 256 + b4 * 131072, xo0 = b * 61440, lw = b4 * 4096, lx = b4 * 6144;
    unsigned sw0 = wo0 + stage_of(0) * 524288, sx0 = xo0 + stage_of(0) * 128;
    asm volatile("" : "+v"(wv[0]), "+v"(wv[1]), "+v"(xv[0]), "+v"(xv[1]));
    asm volatile("" : "+s"(sw0), "+s"(sx0), "+s"(lw), "+s"(lx));
    asm volatile("s_waitcnt lgkmcnt(0)" : "+s"(a01), "+s"(a23), "+s"(a4), "+s"(aowB) : "s"(aowA), "s"(warm) : "memory");
    aow = aowB;
    const rsrc_t rsX = mk_rsrc(mkptr(a01[0], a01[1]), 16 * 48 * 640 * 2);
    const rsrc_t rsW = mk_rsrc(mkptr(a01[2], a01[3]), 640 * 4096 * 2);
    auto issue_A = [&](unsigned sw, unsigned sx, unsigned mw, unsigned mx) {
#pragma unroll
      for (int j = 0; j < 4; ++j) dma16(mw, j * 1024, wv[j >> 1], rsW, sw + j * 32768);
#pragma unroll
      for (int j = 0; j < 6; ++j) dma16(mx, 16384 + j * 1024, xv[j & 1], rsX, sx + j * 10240);
    };
    issue_A(sw0, sx0, lw, lx);
    ws_setup();
    epoch_issue();
    LANE_DEFS
    unsigned vv[2], kvo[4], wov[5];
    const rsrc_t rsK = mk_rsrc((const u16*)mkptr(a23[0], a23[1]) + (b * 32 + h) * 48 * 128, 12288);
    rsrc_t rsV, rsWo, rsPr;
#pragma unroll
    for (int e = 0; e < 4; ++e) kvo[e] = (4 * e + g) * 256 + ((m ^ (4 * e + g)) * 16);
    auto head_of = [&](int i) { return i ^ (c4 >> 1); };
    auto issue_k = [&](unsigned lb, int kt) {
#pragma unroll
      for (int e = 0; e < 4; ++e) dma16(lw, lb + e * 1024, kvo[e], rsK, kt * 4096);
    };
    unsigned two[2], tvo[2];
    auto issue_Bp = [&](int t, int i0, int i1) {
      unsigned hh = head_of(t >> 1);
      asm("" : "+s"(hh));
      const unsigned lb = ((t + 2) & 3) * UNIT_B;
#pragma unroll
      for (int i = i0; i < i1; ++i) {
        const int j = i - 5;
        if (i < 5) dma16(b4 * 5120, lb + i * 1024, wov[i], rsWo, hh * 163840 + two[t & 1]);
        else dma16(b4 * 3072, lb + 20480 + j * 1024, vv[j & 1], rsV, hh * 12288 + tvo[t & 1] + j * 2048);
      }
    };
    int b0 = 3 + (j8 + bg) % 3 * 2;
    unsigned sb = 2 * SLOT_B, sw = wo0 + stage_of(2) * 524288, sx = xo0 + stage_of(2) * 128;
    unsigned mw = sb + lw, mx = sb + lx;
    asm volatile("" : "+v"(kvo[0]), "+v"(kvo[1]), "+v"(kvo[2]), "+v"(kvo[3]));
    asm volatile("" : "+s"(sw), "+s"(sx), "+s"(mw), "+s"(mx), "+s"(b0) : "s"(rsK));
    issue_A(wo0 + stage_of(1) * 524288, xo0 + stage_of(1) * 128, SLOT_B + lw, SLOT_B + lx);
    epoch_done();
#pragma unroll 1
    for (int n = 0; n < 8; ++n) {
      asm volatile("" : "+s"(sw), "+s"(sx), "+s"(mw), "+s"(mx));
      asm volatile("s_cmp_lt_u32 %0, 2\n\ts_cbranch_scc1 .Lkw%=\n\ts_waitcnt vmcnt(10)\n.Lkw%=:\n\ts_waitcnt vmcnt(18)\n\ts_barrier"
                   :: "s"(n + 1 - b0) : "memory", "scc");
      issue_A(sw, sx, mw, mx);
      if (n + 2 == b0) { issue_k(KVBUF, 0); issue_k(KVBUF + 16384, 1); }
      sb = (sb == 2 * SLOT_B) ? 0u : sb + SLOT_B;
      const int nn = stage_of(n + 3);
      sw = wo0 + nn * 524288; sx = xo0 + nn * 128; mw = sb + lw; mx = sb + lx;
    }
    wait_bar<10>();
    issue_k(K2BUF, 2);
    {
      const u32x4 o0 = ldsread16(OFFBUF + b4 * 2048 + lane * 16), o1 = ldsread16(OFFBUF + b4 * 2048 + 1024 + lane * 16);
      wov[0] = o0[0]; wov[1] = o0[1]; wov[2] = o0[2]; wov[3] = o0[3]; wov[4] = o1[0]; vv[0] = o1[1]; vv[1] = o1[2];
      asm volatile("" : "+s"(a23), "+s"(a4), "+s"(ws));
      rsV = mk_rsrc((const u16*)mkptr(a23[2], a23[3]) + (b2 * 32 + hg * 8) * 48 * 128, 8 * 12288);
      rsWo = mk_rsrc((const u16*)mkptr(a4[0], a4[1]) + (hg * 1024) * 640 + cq * 160, 1024 * 1280 - cq * 320);
      rsPr = mk_rsrc(ws + (size_t)((br * 16 + b2) * 32 + hg * 8) * 6144, 8 * 6144);
      unsigned u1 = c4 & 1;
      asm volatile("" : "+s"(u1));
#pragma unroll
      for (int f = 0; f < 2; ++f) {
        const unsigned th = f ^ u1;
        two[f] = (64 * th + 32 * bb + 4 * nh) * 1280;
        tvo[f] = 3 * nh * 2048 + th * 128;
      }
      asm volatile("" : "+s"(two[0]), "+s"(two[1]), "+s"(tvo[0]), "+s"(tvo[1]) : "s"(rsV), "s"(rsWo), "s"(rsPr));
    }
    wait_bar<4>();
    issue_Bp(0, 0, 8);
    wait_bar<8>();
    issue_Bp(1, 0, 8);
    issue_Bp(2, 0, 8);
    wait_bar<16>();
    u32x4 r[6];
    const unsigned pv0 = lane * 16, pv1 = lane * 16 + 4096;
#define PLD "buffer_load_dwordx4 %0, %6, %8, %9 offen sc1\n\tbuffer_load_dwordx4 %1, %6, %8, %9 offen offset:1024 sc1\n\t" \
            "buffer_load_dwordx4 %2, %6, %8, %9 offen offset:2048 sc1\n\tbuffer_load_dwordx4 %3, %6, %8, %9 offen offset:3072 sc1\n\t" \
            "buffer_load_dwordx4 %4, %7, %8, %9 offen sc1\n\tbuffer_load_dwordx4 %5, %7, %8, %9 offen offset:1024 sc1"
#define RT6 "+v"(r[0]), "+v"(r[1]), "+v"(r[2]), "+v"(r[3]), "+v"(r[4]), "+v"(r[5])
    const unsigned pab = PA_AREA + bb * 3072 + pv0, pcb = PC_AREA + bb * 3072 + 8 * nh + pv0;
    static_for<0, 16>([&](auto TC) {
      constexpr int t = decltype(TC)::value, k = t >> 2;
      const unsigned poff = head_of(2 * k + nh) * 6144;
      auto iss = [&](int i0, int i1) { if constexpr (t < 13) issue_Bp(t + 3, i0, i1); };
      if constexpr (t == 15) {
        const unsigned now = (unsigned)__builtin_amdgcn_s_memrealtime();
        __builtin_amdgcn_raw_buffer_store_b64(u32x2{now, tag}, rsP, lane * 64, 40960 + hg * 8, AUX_SC1);
      }
      if constexpr (t == 3 || (t > 3 && (t & 3) == 2)) {
        asm volatile("s_waitcnt vmcnt(%0)" :: "i"(t == 3 ? 0 : t == 14 ? 8 : 16) : "memory");
        iss(0, 1);
        asm volatile("" : RT6);
        auto bad = [&]() {
          unsigned x = __builtin_amdgcn_bitop3_b32(r[0][3], r[1][3], tag, 0x7e);
          unsigned y = __builtin_amdgcn_bitop3_b32(r[2][3], r[3][3], tag, 0x7e);
          unsigned z = __builtin_amdgcn_bitop3_b32(r[4][3], r[5][3], tag, 0x7e);
          return __builtin_amdgcn_ballot_w64((x | y | z) != 0u) != 0ull;
        };
        const bool b_ = bad();
        iss(1, 2);
        if (__builtin_expect(b_, 0)) {
#pragma unroll 1
          for (unsigned spins = 0; spins < SPIN_CAP; ++spins) {
            __builtin_amdgcn_s_sleep(4);
            asm volatile(PLD "\n\ts_waitcnt vmcnt(0)" : RT6 : "v"(pv0), "v"(pv1), "s"(rsPr), "s"(poff) : "memory");
            if (!bad()) break;
          }
        }
        const unsigned ab = pab + (nh ? (2 * k + 1) % 3 : (2 * k) % 3) * 6144;
#pragma unroll
        for (int mt = 0; mt < 3; ++mt) {
          r[2 * mt][3] = r[2 * mt + 1][2];
          *(LAS u32x2*)(pcb + (k & 1) * 6144 + mt * 1024) = u32x2{r[2 * mt + 1][0], r[2 * mt + 1][1]};
          ldswrite16(ab + mt * 1024, r[2 * mt]);
          iss(2 + mt, 3 + mt);
        }
        iss(5, 8);
      } else {
        if constexpr (t > 3 && (t & 3) == 0) asm volatile(PLD : RT6 : "v"(pv0), "v"(pv1), "s"(rsPr), "s"(poff) : "memory");
        iss(0, 8);
        if constexpr (t == 0) {
          const u32x4 z = {0u, 0u, 0u, 0u};
#pragma unroll
          for (int i = 0; i < 3; ++i) ldswrite16((nh ? PC_AREA + 6144 : PA_AREA + 2 * 6144) + bb * 3072 + i * 1024 + lane * 16, z);
        }
        if constexpr (t == 2)
          asm volatile(PLD : "=&v"(r[0]), "=&v"(r[1]), "=&v"(r[2]), "=&v"(r[3]), "=&v"(r[4]), "=&v"(r[5])
                       : "v"(pv0), "v"(pv1), "s"(rsPr), "s"(poff) : "memory");
      }
      wait_bar<t == 2 ? 22 : t < 13 ? 16 : t == 13 ? 8 : t == 14 ? 0 : 63>();
    });
#pragma unroll
    for (int e = 0; e < 12; ++e) own[e] = 0.f;
    hint = u32x2{0u, tag - 1u};
    __builtin_amdgcn_s_sleep(16);
  }
  const int ln = fresh_lane();
  if (hf == 0 || b4 == hg) {
    const int ua = hf ? 4 : hg;
    const unsigned pend = hf ? 15u : 15u & ~(1u << hg);
    unsigned so[4], tg0 = hf ? tag : 0u, l16f = ln * 16, oo = ua * 32;
#pragma unroll
    for (int j = 0; j < 4; ++j) so[j] = ua * 8192 + ((hg + j) & 3) * 2048;
    if (!hf) so[0] = 0x80000000u;
    const rsrc_t rsOut = mk_rsrc(mkptr(aow[0], aow[1]), 768 * 1280 * 2);
    unsigned lo_ = ((b2 * 48 + (ln & 15)) * 1280 + br * 640 + c8 * 80 + 4 * (ln >> 4)) * 2;
    asm volatile("" : "+v"(lo_), "+v"(l16f));
    asm volatile("" : "+s"(so[0]), "+s"(so[1]), "+s"(so[2]), "+s"(so[3]), "+s"(tg0), "+s"(oo) : "s"(rsOut));
#pragma unroll 1
    for (unsigned spins = 0; spins < SPIN_CAP; ++spins) {
      if (((unsigned)__builtin_amdgcn_ballot_w64(hint[1] == tag) & pend) == pend) break;
      __builtin_amdgcn_s_sleep(4);
      asm volatile("buffer_load_dwordx2 %0, %1, %2, 0 offen sc1\n\ts_waitcnt vmcnt(0)" : "+v"(hint) : "v"(ln * 8 + 40960), "s"(rsP) : "memory");
    }
    {
      const unsigned now = (unsigned)__builtin_amdgcn_s_memrealtime();
      int dmax = 0;
#pragma unroll
      for (int k = 0; k < 4; ++k) {
        const int dk = (int)(__builtin_amdgcn_readlane(hint[0], k) - now) + HD - (hf ? HDB : HDU * ((k - hg - 1) & 3));
        if ((pend >> k & 1u) && dk > dmax) dmax = dk;
      }
      dmax = dmax > 2 * HD ? 2 * HD : dmax;
#pragma unroll 1
      for (unsigned spins = 0; spins < SPIN_CAP; ++spins) {
        if ((int)((unsigned)__builtin_amdgcn_s_memrealtime() - now) >= dmax) break;
        __builtin_amdgcn_s_sleep(1);
      }
    }
    f32x2 acc[6];
#pragma unroll 1
    for (unsigned spins = 0;; ++spins) {
      u32x4 c[4][2];
      asm volatile("buffer_load_dwordx4 %0, %8, %9, %10 offen sc1\n\tbuffer_load_dwordx4 %1, %8, %9, %10 offen offset:1024 sc1\n\t"
                   "buffer_load_dwordx4 %2, %8, %9, %11 offen sc1\n\tbuffer_load_dwordx4 %3, %8, %9, %11 offen offset:1024 sc1\n\t"
                   "buffer_load_dwordx4 %4, %8, %9, %12 offen sc1\n\tbuffer_load_dwordx4 %5, %8, %9, %12 offen offset:1024 sc1\n\t"
                   "buffer_load_dwordx4 %6, %8, %9, %13 offen sc1\n\tbuffer_load_dwordx4 %7, %8, %9, %13 offen offset:1024 sc1"
                   : "=&v"(c[0][0]), "=&v"(c[0][1]), "=&v"(c[1][0]), "=&v"(c[1][1]), "=&v"(c[2][0]), "=&v"(c[2][1]), "=&v"(c[3][0]), "=&v"(c[3][1])
                   : "v"(l16f), "s"(rsP), "s"(so[0]), "s"(so[1]), "s"(so[2]), "s"(so[3]) : "memory");
      unsigned bad = 0;
      static_for<0, 4>([&](auto JC) {
        constexpr int j = decltype(JC)::value;
        asm volatile("s_waitcnt vmcnt(%2)" : "+v"(c[j][0]), "+v"(c[j][1]) : "i"(6 - 2 * j) : "memory");
        bad |= __builtin_amdgcn_bitop3_b32(c[j][0][3], c[j][1][3], j ? tag : tg0, 0x7e);
#pragma unroll
        for (int e = 0; e < 6; ++e) {
          const unsigned x = c[j][e / 3][e % 3];
          const f32x2 v = {__uint_as_float(x << 16), __uint_as_float(x & 0xffff0000u)};
          acc[e] = (j ? acc[e] : f32x2{own[2 * e], own[2 * e + 1]}) + v;
        }
        asm volatile("" : "+v"(acc[0]), "+v"(acc[1]), "+v"(acc[2]), "+v"(acc[3]), "+v"(acc[4]), "+v"(acc[5]));
      });
      if (__builtin_expect(__builtin_amdgcn_ballot_w64(bad != 0u) == 0ull || spins >= SPIN_CAP, 1)) break;
      __builtin_amdgcn_s_sleep(4);
    }
#pragma unroll
    for (int mt = 0; mt < 3; ++mt)
      __builtin_amdgcn_raw_buffer_store_b64(u32x2{cvtpk(acc[2 * mt][0], acc[2 * mt][1]), cvtpk(acc[2 * mt + 1][0], acc[2 * mt + 1][1])}, rsOut, lo_, oo + mt * 16 * 2560, AUX_SC1);
  }
  if (h == 0 && w == 0 && ln == 0)
    __hip_atomic_store((GAS unsigned*)(ctrl + (br * 4 + bg) * 256), tag, __ATOMIC_RELAXED, __HIP_MEMORY_SCOPE_AGENT);
}

extern "C" void kernel_launch(void* const* d_in, const int* in_sizes, int n_in, void* d_out, int out_size, void* d_ws,
                              size_t ws_size, hipStream_t stream) {
  Params P;
  for (int i = 0; i < 2; ++i) {
    P.in[i][0] = (const u16*)d_in[5 * i + 0];
    P.in[i][1] = (const u16*)d_in[5 * i + 3];
    P.in[i][2] = (const u16*)d_in[5 * i + 1];
    P.in[i][3] = (const u16*)d_in[5 * i + 2];
    P.in[i][4] = (const u16*)d_in[5 * i + 4];
  }
  P.out = (u16*)d_out;
  P.ws = (char*)d_ws;
  (void)hipFuncSetAttribute((const void*)fused_kernel, hipFuncAttributeMaxDynamicSharedMemorySize, 163840);
  hipLaunchKernelGGL(fused_kernel, dim3(256), dim3(512), 163840, stream, P);
}
```
